```python
import torch

def _softmax(x, axis=-1):
    return torch.softmax(x, dim=axis)
def _gelu(x):
    return torch.nn.functional.gelu(x, approximate="tanh")
def _silu(x):
    return torch.nn.functional.silu(x)
def _sigmoid(x):
    return torch.sigmoid(x)
def _layernorm(x, axis=-1, eps=1e-5):
    m = x.mean(dim=axis, keepdim=True)
    v = x.var(dim=axis, keepdim=True, unbiased=False)
    return (x - m) / torch.sqrt(v + eps)
def _scatter_add(tgt, idx, src, axis=0):
    return tgt.clone().index_add_(axis, idx, src)
def _segment_sum(src, seg, n):
    out = torch.zeros((n,) + src.shape[1:], dtype=src.dtype)
    return out.index_add_(0, seg, src)
def _pad(x, pw):
    flat = tuple(v for pair in reversed(pw) for v in pair)
    return torch.nn.functional.pad(x, flat)

kernel_name = 'standalone__gqa_block_cached__near_ridge'

def setup_inputs():

    _w = (lambda a: (lambda t: t.to(torch.bfloat16) if t.is_floating_point() else t)(torch.as_tensor(a)))
    B, Sq, D = 16, 80, 512
    Hq, Hkv, Sk, Dh = 32, 8, 1024, 128
    assert Hq % Hkv == 0, (Hq, Hkv)
    return {
        "x": _w((lambda *s: torch.randn(*s, dtype=torch.bfloat16) * 0.8)(B, Sq, D)),
        "k_cache": _w((lambda *s: torch.randn(*s, dtype=torch.bfloat16) * 0.8)(B, Hkv, Sk, Dh)),
        "v_cache": _w((lambda *s: torch.randn(*s, dtype=torch.bfloat16) * 0.05)(B, Hkv, Sk, Dh)),
        "wq": _w((lambda *s: torch.randn(*s, dtype=torch.bfloat16) * 0.8)(D, Hq * Dh) * (D ** -0.5)),
        "wo": _w((lambda *s: torch.randn(*s, dtype=torch.bfloat16) * 0.05)(Hq * Dh, D) * ((Hq * Dh) ** -0.5)),
    }

def reference(x, k_cache, v_cache, wq, wo):
    B, Sq, D = x.shape
    Hq, Dh = 32, 128
    Hkv = k_cache.shape[1]
    rep = Hq // Hkv
    q = (lambda x, *d: x.permute(*d))((x @ wq).reshape(B, Sq, Hq, Dh), 0, 2, 1, 3)
    k = (lambda x, n, axis: torch.repeat_interleave(x, n, dim=axis))(k_cache, rep, axis=1)
    v = (lambda x, n, axis: torch.repeat_interleave(x, n, dim=axis))(v_cache, rep, axis=1)
    scores = torch.einsum("bhqd,bhkd->bhqk", q, k) * (Dh ** -0.5)
    p = _softmax(scores, axis=-1)
    o = torch.einsum("bhqk,bhkd->bhqd", p, v)
    return (lambda x, *d: x.permute(*d))(o, 0, 2, 1, 3).reshape(B, Sq, Hq * Dh) @ wo
```

```cpp
#include <hip/hip_runtime.h>
#include <hip/hip_bf16.h>
#include <stdint.h>

typedef __attribute__((ext_vector_type(8))) short bf16x8;
typedef __attribute__((ext_vector_type(4))) short bf16x4;
typedef __attribute__((ext_vector_type(4))) float f32x4;
typedef __attribute__((ext_vector_type(4))) unsigned u32x4;
typedef __attribute__((ext_vector_type(2))) unsigned u32x2;
typedef unsigned short u16;

#define NB 16
#define SQ 80
#define DM 512
#define HQ 32
#define HKV 8
#define SK 1024
#define DH 128

#define LDS_AS __attribute__((address_space(3)))

__device__ __forceinline__ unsigned cvtpk(float lo, float hi) {
  unsigned r;
  asm volatile("v_cvt_pk_bf16_f32 %0, %1, %2" : "=v"(r) : "v"(lo), "v"(hi));
  return r;
}

__device__ __forceinline__ bf16x4 tr_read(const char* p) {
  return __builtin_amdgcn_ds_read_tr16_b64_v4i16((LDS_AS bf16x4*)(p));
}

template <int NT, int MODE>
__global__ __launch_bounds__(256) void gemm80(const u16* __restrict__ A, const u16* __restrict__ W,
                                               u16* __restrict__ C, int K, int N) {
  constexpr int BN = 64 * NT;
  __shared__ __attribute__((aligned(16))) char smem[80 * 128 + 64 * BN * 2];
  char* xs = smem;
  char* ws = smem + 80 * 128;
  const int tid = threadIdx.x, w = tid >> 6, l = tid & 63, g = l >> 4, li = l & 15;
  const int b = blockIdx.y, n0 = blockIdx.x * BN;
  const u16* Ab = A + (size_t)b * 80 * K;
  f32x4 acc[NT][5];
#pragma unroll
  for (int i = 0; i < NT; ++i)
#pragma unroll
    for (int j = 0; j < 5; ++j) acc[i][j] = f32x4{0.f, 0.f, 0.f, 0.f};
  for (int k0 = 0; k0 < K; k0 += 64) {
    __syncthreads();
    for (int c = tid; c < 640; c += 256) {
      int row = c >> 3, ch = c & 7;
      u32x4 v = *(const u32x4*)(Ab + (size_t)row * K + k0 + ch * 8);
      *(u32x4*)(xs + row * 128 + ((ch ^ (row & 7)) << 4)) = v;
    }
    for (int c = tid; c < 64 * BN / 8; c += 256) {
      int row = c / (BN / 8), ch = c % (BN / 8);
      u32x4 v = *(const u32x4*)(W + (size_t)(k0 + row) * N + n0 + ch * 8);
      *(u32x4*)(ws + row * (BN * 2) + ch * 16) = v;
    }
    __syncthreads();
#pragma unroll
    for (int kk = 0; kk < 2; ++kk) {
      bf16x8 bfr[5];
#pragma unroll
      for (int j = 0; j < 5; ++j) {
        int row = j * 16 + li;
        bfr[j] = *(const bf16x8*)(xs + row * 128 + (((kk * 4 + g) ^ (row & 7)) << 4));
      }
#pragma unroll
      for (int i = 0; i < NT; ++i) {
        int col = (w * NT + i) * 16 + (li & 3) * 4;
        int r0 = kk * 32 + 8 * g + (li >> 2);
        bf16x4 a0 = tr_read(ws + r0 * (BN * 2) + col * 2);
        bf16x4 a1 = tr_read(ws + (r0 + 4) * (BN * 2) + col * 2);
        bf16x8 afr = {a0[0], a0[1], a0[2], a0[3], a1[0], a1[1], a1[2], a1[3]};
#pragma unroll
        for (int j = 0; j < 5; ++j)
          acc[i][j] = __builtin_amdgcn_mfma_f32_16x16x32_bf16(afr, bfr[j], acc[i][j], 0, 0, 0);
      }
    }
  }
#pragma unroll
  for (int i = 0; i < NT; ++i)
#pragma unroll
    for (int j = 0; j < 5; ++j) {
      int row = j * 16 + li;
      int col = n0 + (w * NT + i) * 16 + 4 * g;
      u32x2 o = {cvtpk(acc[i][j][0], acc[i][j][1]), cvtpk(acc[i][j][2], acc[i][j][3])};
      if (MODE == 0) {
        int h = col >> 7, d = col & 127;
        *(u32x2*)(C + ((size_t)(b * HQ + h) * SQ + row) * DH + d) = o;
      } else {
        *(u32x2*)(C + ((size_t)b * SQ + row) * N + col) = o;
      }
    }
}

#define SCL2E (0.08838834764831845f * 1.4426950408889634f)

__device__ __forceinline__ void stage_kv(const u16* kq, const u16* vq, char* buf, int j, int tid) {
#pragma unroll
  for (int i = 0; i < 8; ++i) {
    const u16* src = (i < 4 ? kq : vq) + (((i >> 1) & 1) * 512 + j * 64 + (i & 1) * 32) * DH;
    __builtin_amdgcn_global_load_lds((const unsigned*)src, (LDS_AS unsigned*)(buf + (i * 512 + tid) * 16), 16, 0, 0);
  }
}

template <int NQ>
__device__ __forceinline__ void attn_body(const u16* __restrict__ Qw, const u16* __restrict__ kp,
                                          const u16* __restrict__ vp, u16* __restrict__ Ow, char* smem, int b, int h,
                                          int kh, int q0, int pairslot) {
  const int tid = threadIdx.x, l = tid & 63, g = l >> 4, li = l & 15;
  bf16x8 qf[NQ][4];
#pragma unroll
  for (int n = 0; n < NQ; ++n)
#pragma unroll
    for (int s = 0; s < 4; ++s)
      qf[n][s] = *(const bf16x8*)(Qw + ((size_t)(b * HQ + h) * SQ + q0 + 16 * n + li) * DH + 32 * s + 8 * g);
  f32x4 o[8][NQ];
#pragma unroll
  for (int d = 0; d < 8; ++d)
#pragma unroll
    for (int n = 0; n < NQ; ++n) o[d][n] = f32x4{0.f, 0.f, 0.f, 0.f};
  float m[NQ], ls[NQ];
#pragma unroll
  for (int n = 0; n < NQ; ++n) { m[n] = -1e30f; ls[n] = 0.f; }

  const u16* kq = kp + (tid >> 4) * DH + (((tid & 15) ^ ((tid >> 4) & 15)) << 3);
  const u16* vq = vp + (tid >> 4) * DH + (((tid & 15) ^ (((tid >> 4) & 7) << 1)) << 3);
  stage_kv(kq, vq, smem, 0, tid);
  const int koff = kh * 16384 + li * 256;
  const int voff = 32768 + kh * 16384;
  for (int j = 0; j < 8; ++j) {
    __syncthreads();
    if (j + 1 < 8) stage_kv(kq, vq, smem + ((j + 1) & 1) * 65536, j + 1, tid);
    const char* kb = smem + (j & 1) * 65536 + koff;
    const char* vb = smem + (j & 1) * 65536 + voff;
#pragma unroll
    for (int hb = 0; hb < 2; ++hb) {
      f32x4 s[2][NQ];
#pragma unroll
      for (int kt = 0; kt < 2; ++kt) {
#pragma unroll
        for (int n = 0; n < NQ; ++n) s[kt][n] = f32x4{0.f, 0.f, 0.f, 0.f};
#pragma unroll
        for (int ks = 0; ks < 4; ++ks) {
          bf16x8 a = *(const bf16x8*)(kb + (hb * 2 + kt) * 4096 + (((4 * ks + g) ^ li) << 4));
#pragma unroll
          for (int n = 0; n < NQ; ++n) s[kt][n] = __builtin_amdgcn_mfma_f32_16x16x32_bf16(a, qf[n][ks], s[kt][n], 0, 0, 0);
        }
      }
      bf16x8 pf[NQ];
#pragma unroll
      for (int n = 0; n < NQ; ++n) {
        float mx = s[0][n][0];
#pragma unroll
        for (int kt = 0; kt < 2; ++kt)
#pragma unroll
          for (int r = 0; r < 4; ++r) mx = fmaxf(mx, s[kt][n][r]);
        mx = fmaxf(mx, __shfl_xor(mx, 16));
        mx = fmaxf(mx, __shfl_xor(mx, 32));
        float mn = fmaxf(m[n], mx);
        float alpha = __builtin_amdgcn_exp2f((m[n] - mn) * SCL2E);
        m[n] = mn;
        float nm = -mn * SCL2E;
        float sum = 0.f;
#pragma unroll
        for (int kt = 0; kt < 2; ++kt)
#pragma unroll
          for (int r = 0; r < 4; ++r) {
            float p = __builtin_amdgcn_exp2f(fmaf(s[kt][n][r], SCL2E, nm));
            s[kt][n][r] = p;
            sum += p;
          }
        ls[n] = ls[n] * alpha + sum;
#pragma unroll
        for (int d = 0; d < 8; ++d) o[d][n] *= alpha;
        u32x4 pk = {cvtpk(s[0][n][0], s[0][n][1]), cvtpk(s[0][n][2], s[0][n][3]),
                    cvtpk(s[1][n][0], s[1][n][1]), cvtpk(s[1][n][2], s[1][n][3])};
        pf[n] = __builtin_bit_cast(bf16x8, pk);
      }
#pragma unroll
      for (int d = 0; d < 8; ++d) {
        int row = 32 * hb + 4 * g + (li >> 2);
        int sw = ((row & 7) << 1);
        int ch = 2 * d + ((li & 3) >> 1);
        const char* a = vb + row * 256 + ((ch ^ sw) << 4) + 8 * (li & 1);
        bf16x4 a0 = tr_read(a);
        bf16x4 a1 = tr_read(a + 16 * 256);
        bf16x8 af = {a0[0], a0[1], a0[2], a0[3], a1[0], a1[1], a1[2], a1[3]};
#pragma unroll
        for (int n = 0; n < NQ; ++n) o[d][n] = __builtin_amdgcn_mfma_f32_16x16x32_bf16(af, pf[n], o[d][n], 0, 0, 0);
      }
    }
  }
  int tz = threadIdx.x;
  asm volatile("" : "+v"(tz));
  const int l2 = tz & 63, g2 = l2 >> 4, li2 = l2 & 15;
#pragma unroll
  for (int n = 0; n < NQ; ++n) {
    ls[n] += __shfl_xor(ls[n], 16);
    ls[n] += __shfl_xor(ls[n], 32);
  }
  __syncthreads();
  char* xb = smem + pairslot * 32768;
  if (kh == 1) {
#pragma unroll
    for (int n = 0; n < NQ; ++n) {
#pragma unroll
      for (int d = 0; d < 8; ++d) *(f32x4*)(xb + ((n * 8 + d) * 64 + l2) * 16) = o[d][n];
      *(float*)(xb + 24576 + (n * 2) * 256 + l2 * 4) = m[n];
      *(float*)(xb + 24576 + (n * 2 + 1) * 256 + l2 * 4) = ls[n];
    }
  }
  __syncthreads();
  if (kh == 0) {
#pragma unroll
    for (int n = 0; n < NQ; ++n) {
      float m1 = *(const float*)(xb + 24576 + (n * 2) * 256 + l2 * 4);
      float l1 = *(const float*)(xb + 24576 + (n * 2 + 1) * 256 + l2 * 4);
      float mm = fmaxf(m[n], m1);
      float a0 = __builtin_amdgcn_exp2f((m[n] - mm) * SCL2E), a1 = __builtin_amdgcn_exp2f((m1 - mm) * SCL2E);
      float lt = ls[n] * a0 + l1 * a1;
      float inv = 1.f / lt;
      a0 *= inv;
      a1 *= inv;
      u16* op = Ow + ((size_t)b * SQ + q0 + 16 * n + li2) * (HQ * DH) + h * DH + 4 * g2;
#pragma unroll
      for (int d = 0; d < 8; ++d) {
        f32x4 o1 = *(const f32x4*)(xb + ((n * 8 + d) * 64 + l2) * 16);
        f32x4 r = o[d][n] * a0 + o1 * a1;
        u32x2 pk = {cvtpk(r[0], r[1]), cvtpk(r[2], r[3])};
        *(u32x2*)(op + 16 * d) = pk;
      }
    }
  }
}

__global__ __launch_bounds__(512, 2) void attn_kernel(const u16* __restrict__ Qw, const u16* __restrict__ Kc,
                                                      const u16* __restrict__ Vc, u16* __restrict__ Ow) {
  __shared__ __attribute__((aligned(16))) char smem[131072];
  const int bid = blockIdx.x;
  const int xcd = bid & 7, idx = bid >> 3;
  const int pair = xcd * 16 + (idx >> 1), hp = idx & 1;
  const int b = pair >> 3, kvh = pair & 7;
  const int w = __builtin_amdgcn_readfirstlane(threadIdx.x >> 6);
  const int hsel = w & 1, kh = (w >> 1) & 1, part = w >> 2;
  const int h = kvh * 4 + hp * 2 + hsel;
  const u16* kp = Kc + (size_t)(b * HKV + kvh) * SK * DH;
  const u16* vp = Vc + (size_t)(b * HKV + kvh) * SK * DH;
  if (part == 0) attn_body<3>(Qw, kp, vp, Ow, smem, b, h, kh, 0, hsel);
  else attn_body<2>(Qw, kp, vp, Ow, smem, b, h, kh, 48, 2 + hsel);
}

extern "C" void kernel_launch(void* const* d_in, const int* in_sizes, int n_in, void* d_out, int out_size, void* d_ws,
                              size_t ws_size, hipStream_t stream) {
  const u16* x = (const u16*)d_in[0];
  const u16* kc = (const u16*)d_in[1];
  const u16* vc = (const u16*)d_in[2];
  const u16* wq = (const u16*)d_in[3];
  const u16* wo = (const u16*)d_in[4];
  u16* out = (u16*)d_out;
  u16* Qw = (u16*)d_ws;
  u16* Ow = Qw + (size_t)NB * SQ * HQ * DH;
  gemm80<2, 0><<<dim3(32, NB), 256, 0, stream>>>(x, wq, Qw, DM, HQ * DH);
  attn_kernel<<<256, 512, 0, stream>>>(Qw, kc, vc, Ow);
  gemm80<1, 1><<<dim3(8, NB), 256, 0, stream>>>(Ow, wo, out, HQ * DH, DM);
}
```

```cpp
#include <hip/hip_runtime.h>
#include <hip/hip_bf16.h>
#include <stdint.h>

typedef __attribute__((ext_vector_type(8))) short bf16x8;
typedef __attribute__((ext_vector_type(4))) short bf16x4;
typedef __attribute__((ext_vector_type(4))) float f32x4;
typedef __attribute__((ext_vector_type(4))) unsigned u32x4;
typedef __attribute__((ext_vector_type(2))) unsigned u32x2;
typedef unsigned short u16;

#define NB 16
#define SQ 80
#define DM 512
#define HQ 32
#define HKV 8
#define SK 1024
#define DH 128

#define LDS_AS __attribute__((address_space(3)))

__device__ __forceinline__ unsigned cvtpk(float lo, float hi) {
  unsigned r;
  asm volatile("v_cvt_pk_bf16_f32 %0, %1, %2" : "=v"(r) : "v"(lo), "v"(hi));
  return r;
}

__device__ __forceinline__ bf16x4 tr_read(const char* p) {
  return __builtin_amdgcn_ds_read_tr16_b64_v4i16((LDS_AS bf16x4*)(p));
}

template <int MODE>
__global__ __launch_bounds__(256) void gemm160(const u16* __restrict__ A, const u16* __restrict__ W,
                                               void* __restrict__ Cv, int K, int N, int KS) {
  __shared__ __attribute__((aligned(16))) char smem[2 * 28672];
  const int tid = threadIdx.x, w = tid >> 6, l = tid & 63, g = l >> 4, li = l & 15;
  const int wm = w >> 1, wn = w & 1;
  const int m0 = blockIdx.x * 160, n0 = blockIdx.y * 64, kb0 = blockIdx.z * KS;
  const u16* asrc[5];
#pragma unroll
  for (int i = 0; i < 5; ++i) {
    int c = i * 256 + tid, row = c >> 3, pos = c & 7;
    asrc[i] = A + (size_t)(m0 + row) * K + kb0 + ((pos ^ (row & 7)) << 3);
  }
  const u16* wsrc[2];
#pragma unroll
  for (int i = 0; i < 2; ++i) {
    int c = i * 256 + tid, row = c >> 3, pos = c & 7;
    wsrc[i] = W + (size_t)(kb0 + row) * N + n0 + ((pos ^ (((row >> 1) & 3) << 1)) << 3);
  }
  auto stage = [&](int kt, char* buf) {
#pragma unroll
    for (int i = 0; i < 5; ++i)
      __builtin_amdgcn_global_load_lds((const unsigned*)(asrc[i] + kt * 64), (LDS_AS unsigned*)(buf + (i * 256 + tid) * 16), 16, 0, 0);
#pragma unroll
    for (int i = 0; i < 2; ++i)
      __builtin_amdgcn_global_load_lds((const unsigned*)(wsrc[i] + (size_t)kt * 64 * N), (LDS_AS unsigned*)(buf + 20480 + (i * 256 + tid) * 16), 16, 0, 0);
  };
  f32x4 acc[2][5];
#pragma unroll
  for (int i = 0; i < 2; ++i)
#pragma unroll
    for (int j = 0; j < 5; ++j) acc[i][j] = f32x4{0.f, 0.f, 0.f, 0.f};
  const int nt = KS / 64;
  stage(0, smem);
  for (int kt = 0; kt < nt; ++kt) {
    __syncthreads();
    if (kt + 1 < nt) stage(kt + 1, smem + ((kt + 1) & 1) * 28672);
    const char* as = smem + (kt & 1) * 28672;
    const char* ws = as + 20480;
#pragma unroll
    for (int kk = 0; kk < 2; ++kk) {
      bf16x8 bfr[5];
#pragma unroll
      for (int j = 0; j < 5; ++j) {
        int row = wm * 80 + j * 16 + li;
        bfr[j] = *(const bf16x8*)(as + row * 128 + (((kk * 4 + g) ^ (row & 7)) << 4));
      }
#pragma unroll
      for (int i = 0; i < 2; ++i) {
        int r0 = kk * 32 + 8 * g + (li >> 2);
        int ch = wn * 4 + i * 2 + ((li & 3) >> 1);
        const char* p0 = ws + r0 * 128 + ((ch ^ (((r0 >> 1) & 3) << 1)) << 4) + 8 * (li & 1);
        const char* p1 = ws + (r0 + 4) * 128 + ((ch ^ ((((r0 + 4) >> 1) & 3) << 1)) << 4) + 8 * (li & 1);
        bf16x4 a0 = tr_read(p0);
        bf16x4 a1 = tr_read(p1);
        bf16x8 afr = {a0[0], a0[1], a0[2], a0[3], a1[0], a1[1], a1[2], a1[3]};
#pragma unroll
        for (int j = 0; j < 5; ++j)
          acc[i][j] = __builtin_amdgcn_mfma_f32_16x16x32_bf16(afr, bfr[j], acc[i][j], 0, 0, 0);
      }
    }
  }
#pragma unroll
  for (int i = 0; i < 2; ++i)
#pragma unroll
    for (int j = 0; j < 5; ++j) {
      int row = m0 + wm * 80 + j * 16 + li;
      int col = n0 + wn * 32 + i * 16 + 4 * g;
      if (MODE == 0) {
        u32x2 o = {cvtpk(acc[i][j][0], acc[i][j][1]), cvtpk(acc[i][j][2], acc[i][j][3])};
        int b = row / SQ, q = row % SQ, h = col >> 7, d = col & 127;
        *(u32x2*)((u16*)Cv + ((size_t)(b * HQ + h) * SQ + q) * DH + d) = o;
      } else {
        *(f32x4*)((float*)Cv + ((size_t)blockIdx.z * NB * SQ + row) * N + col) = acc[i][j];
      }
    }
}

__global__ __launch_bounds__(256) void reduce4(const float* __restrict__ P, u16* __restrict__ out) {
  size_t i = ((size_t)blockIdx.x * 256 + threadIdx.x) * 4;
  const size_t S = (size_t)NB * SQ * DM;
  f32x4 v = *(const f32x4*)(P + i) + *(const f32x4*)(P + S + i) + *(const f32x4*)(P + 2 * S + i) + *(const f32x4*)(P + 3 * S + i);
  u32x2 o = {cvtpk(v[0], v[1]), cvtpk(v[2], v[3])};
  *(u32x2*)(out + i) = o;
}

#define SCL2E (0.08838834764831845f * 1.4426950408889634f)

__device__ __forceinline__ void stage_kv(const u16* kq, const u16* vq, char* buf, int j, int tid) {
#pragma unroll
  for (int i = 0; i < 8; ++i) {
    const u16* src = (i < 4 ? kq : vq) + (((i >> 1) & 1) * 512 + j * 64 + (i & 1) * 32) * DH;
    __builtin_amdgcn_global_load_lds((const unsigned*)src, (LDS_AS unsigned*)(buf + (i * 512 + tid) * 16), 16, 0, 0);
  }
}

template <int NQ>
__device__ __forceinline__ void attn_body(const u16* __restrict__ Qw, const u16* __restrict__ kp,
                                          const u16* __restrict__ vp, u16* __restrict__ Ow, char* smem, int b, int h,
                                          int kh, int q0, int pairslot) {
  const int tid = threadIdx.x, l = tid & 63, g = l >> 4, li = l & 15;
  bf16x8 qf[NQ][4];
#pragma unroll
  for (int n = 0; n < NQ; ++n)
#pragma unroll
    for (int s = 0; s < 4; ++s)
      qf[n][s] = *(const bf16x8*)(Qw + ((size_t)(b * HQ + h) * SQ + q0 + 16 * n + li) * DH + 32 * s + 8 * g);
  f32x4 o[8][NQ];
#pragma unroll
  for (int d = 0; d < 8; ++d)
#pragma unroll
    for (int n = 0; n < NQ; ++n) o[d][n] = f32x4{0.f, 0.f, 0.f, 0.f};
  float m[NQ], ls[NQ];
#pragma unroll
  for (int n = 0; n < NQ; ++n) { m[n] = -1e30f; ls[n] = 0.f; }

  const u16* kq = kp + (tid >> 4) * DH + (((tid & 15) ^ ((tid >> 4) & 15)) << 3);
  const u16* vq = vp + (tid >> 4) * DH + (((tid & 15) ^ (((tid >> 4) & 7) << 1)) << 3);
  stage_kv(kq, vq, smem, 0, tid);
  const int koff = kh * 16384 + li * 256;
  const int voff = 32768 + kh * 16384;
  for (int j = 0; j < 8; ++j) {
    __syncthreads();
    if (j + 1 < 8) stage_kv(kq, vq, smem + ((j + 1) & 1) * 65536, j + 1, tid);
    const char* kb = smem + (j & 1) * 65536 + koff;
    const char* vb = smem + (j & 1) * 65536 + voff;
#pragma unroll
    for (int hb = 0; hb < 2; ++hb) {
      f32x4 s[2][NQ];
#pragma unroll
      for (int kt = 0; kt < 2; ++kt) {
#pragma unroll
        for (int n = 0; n < NQ; ++n) s[kt][n] = f32x4{0.f, 0.f, 0.f, 0.f};
#pragma unroll
        for (int ks = 0; ks < 4; ++ks) {
          bf16x8 a = *(const bf16x8*)(kb + (hb * 2 + kt) * 4096 + (((4 * ks + g) ^ li) << 4));
#pragma unroll
          for (int n = 0; n < NQ; ++n) s[kt][n] = __builtin_amdgcn_mfma_f32_16x16x32_bf16(a, qf[n][ks], s[kt][n], 0, 0, 0);
        }
      }
      bf16x8 pf[NQ];
#pragma unroll
      for (int n = 0; n < NQ; ++n) {
        float mx = s[0][n][0];
#pragma unroll
        for (int kt = 0; kt < 2; ++kt)
#pragma unroll
          for (int r = 0; r < 4; ++r) mx = fmaxf(mx, s[kt][n][r]);
        mx = fmaxf(mx, __shfl_xor(mx, 16));
        mx = fmaxf(mx, __shfl_xor(mx, 32));
        float mn = fmaxf(m[n], mx);
        float alpha = __builtin_amdgcn_exp2f((m[n] - mn) * SCL2E);
        m[n] = mn;
        float nm = -mn * SCL2E;
        float sum = 0.f;
#pragma unroll
        for (int kt = 0; kt < 2; ++kt)
#pragma unroll
          for (int r = 0; r < 4; ++r) {
            float p = __builtin_amdgcn_exp2f(fmaf(s[kt][n][r], SCL2E, nm));
            s[kt][n][r] = p;
            sum += p;
          }
        ls[n] = ls[n] * alpha + sum;
#pragma unroll
        for (int d = 0; d < 8; ++d) o[d][n] *= alpha;
        u32x4 pk = {cvtpk(s[0][n][0], s[0][n][1]), cvtpk(s[0][n][2], s[0][n][3]),
                    cvtpk(s[1][n][0], s[1][n][1]), cvtpk(s[1][n][2], s[1][n][3])};
        pf[n] = __builtin_bit_cast(bf16x8, pk);
      }
#pragma unroll
      for (int d = 0; d < 8; ++d) {
        int row = 32 * hb + 4 * g + (li >> 2);
        int sw = ((row & 7) << 1);
        int ch = 2 * d + ((li & 3) >> 1);
        const char* a = vb + row * 256 + ((ch ^ sw) << 4) + 8 * (li & 1);
        bf16x4 a0 = tr_read(a);
        bf16x4 a1 = tr_read(a + 16 * 256);
        bf16x8 af = {a0[0], a0[1], a0[2], a0[3], a1[0], a1[1], a1[2], a1[3]};
#pragma unroll
        for (int n = 0; n < NQ; ++n) o[d][n] = __builtin_amdgcn_mfma_f32_16x16x32_bf16(af, pf[n], o[d][n], 0, 0, 0);
      }
    }
  }
  int tz = threadIdx.x;
  asm volatile("" : "+v"(tz));
  const int l2 = tz & 63, g2 = l2 >> 4, li2 = l2 & 15;
#pragma unroll
  for (int n = 0; n < NQ; ++n) {
    ls[n] += __shfl_xor(ls[n], 16);
    ls[n] += __shfl_xor(ls[n], 32);
  }
  __syncthreads();
  char* xb = smem + pairslot * 32768;
  if (kh == 1) {
#pragma unroll
    for (int n = 0; n < NQ; ++n) {
#pragma unroll
      for (int d = 0; d < 8; ++d) *(f32x4*)(xb + ((n * 8 + d) * 64 + l2) * 16) = o[d][n];
      *(float*)(xb + 24576 + (n * 2) * 256 + l2 * 4) = m[n];
      *(float*)(xb + 24576 + (n * 2 + 1) * 256 + l2 * 4) = ls[n];
    }
  }
  __syncthreads();
  if (kh == 0) {
#pragma unroll
    for (int n = 0; n < NQ; ++n) {
      float m1 = *(const float*)(xb + 24576 + (n * 2) * 256 + l2 * 4);
      float l1 = *(const float*)(xb + 24576 + (n * 2 + 1) * 256 + l2 * 4);
      float mm = fmaxf(m[n], m1);
      float a0 = __builtin_amdgcn_exp2f((m[n] - mm) * SCL2E), a1 = __builtin_amdgcn_exp2f((m1 - mm) * SCL2E);
      float lt = ls[n] * a0 + l1 * a1;
      float inv = 1.f / lt;
      a0 *= inv;
      a1 *= inv;
      u16* op = Ow + ((size_t)b * SQ + q0 + 16 * n + li2) * (HQ * DH) + h * DH + 4 * g2;
#pragma unroll
      for (int d = 0; d < 8; ++d) {
        f32x4 o1 = *(const f32x4*)(xb + ((n * 8 + d) * 64 + l2) * 16);
        f32x4 r = o[d][n] * a0 + o1 * a1;
        u32x2 pk = {cvtpk(r[0], r[1]), cvtpk(r[2], r[3])};
        *(u32x2*)(op + 16 * d) = pk;
      }
    }
  }
}

__global__ __launch_bounds__(512, 2) void attn_kernel(const u16* __restrict__ Qw, const u16* __restrict__ Kc,
                                                      const u16* __restrict__ Vc, u16* __restrict__ Ow) {
  __shared__ __attribute__((aligned(16))) char smem[131072];
  const int bid = blockIdx.x;
  const int xcd = bid & 7, idx = bid >> 3;
  const int pair = xcd * 16 + (idx >> 1), hp = idx & 1;
  const int b = pair >> 3, kvh = pair & 7;
  const int w = __builtin_amdgcn_readfirstlane(threadIdx.x >> 6);
  const int hsel = w & 1, kh = (w >> 1) & 1, part = w >> 2;
  const int h = kvh * 4 + hp * 2 + hsel;
  const u16* kp = Kc + (size_t)(b * HKV + kvh) * SK * DH;
  const u16* vp = Vc + (size_t)(b * HKV + kvh) * SK * DH;
  if (part == 0) attn_body<3>(Qw, kp, vp, Ow, smem, b, h, kh, 0, hsel);
  else attn_body<2>(Qw, kp, vp, Ow, smem, b, h, kh, 48, 2 + hsel);
}

extern "C" void kernel_launch(void* const* d_in, const int* in_sizes, int n_in, void* d_out, int out_size, void* d_ws,
                              size_t ws_size, hipStream_t stream) {
  const u16* x = (const u16*)d_in[0];
  const u16* kc = (const u16*)d_in[1];
  const u16* vc = (const u16*)d_in[2];
  const u16* wq = (const u16*)d_in[3];
  const u16* wo = (const u16*)d_in[4];
  u16* out = (u16*)d_out;
  u16* Qw = (u16*)d_ws;
  u16* Ow = Qw + (size_t)NB * SQ * HQ * DH;
  float* Pw = (float*)(Ow + (size_t)NB * SQ * HQ * DH);
  gemm160<0><<<dim3(8, 64, 1), 256, 0, stream>>>(x, wq, Qw, DM, HQ * DH, DM);
  attn_kernel<<<256, 512, 0, stream>>>(Qw, kc, vc, Ow);
  gemm160<1><<<dim3(8, 8, 4), 256, 0, stream>>>(Ow, wo, Pw, HQ * DH, DM, 1024);
  reduce4<<<640, 256, 0, stream>>>(Pw, out);
}
```
